```python
import torch

def _softmax(x, axis=-1):
    return torch.softmax(x, dim=axis)
def _gelu(x):
    return torch.nn.functional.gelu(x, approximate="tanh")
def _silu(x):
    return torch.nn.functional.silu(x)
def _sigmoid(x):
    return torch.sigmoid(x)
def _layernorm(x, axis=-1, eps=1e-5):
    m = x.mean(dim=axis, keepdim=True)
    v = x.var(dim=axis, keepdim=True, unbiased=False)
    return (x - m) / torch.sqrt(v + eps)
def _scatter_add(tgt, idx, src, axis=0):
    return tgt.clone().index_add_(axis, idx, src)
def _segment_sum(src, seg, n):
    out = torch.zeros((n,) + src.shape[1:], dtype=src.dtype)
    return out.index_add_(0, seg, src)
def _pad(x, pw):
    flat = tuple(v for pair in reversed(pw) for v in pair)
    return torch.nn.functional.pad(x, flat)

kernel_name = 'recurrent_scan__sliding_block_cached__glu_gated_bilinear__decode'

def setup_inputs():

    _w = (lambda a: (lambda t: t.to(torch.bfloat16) if t.is_floating_point() else t)(torch.as_tensor(a)))
    def _A_setup():
        B, Sq, D = 8, 1, 512
        Hq, Hkv, Sk, Dh = 32, 8, 4096, 128
        assert Hq % Hkv == 0, (Hq, Hkv)
        return {
            "x": _w((lambda *s: torch.randn(*s, dtype=torch.bfloat16) * 0.05)(B, Sq, D)),
            "k_cache": _w((lambda *s: torch.randn(*s, dtype=torch.bfloat16) * 3.2)(B, Hkv, Sk, Dh)),
            "v_cache": _w((lambda *s: torch.randn(*s, dtype=torch.bfloat16) * 3.2)(B, Hkv, Sk, Dh)),
            "wq": _w((lambda *s: torch.randn(*s, dtype=torch.bfloat16) * 3.2)(D, Hq * Dh) * (D ** -0.5)),
            "wo": _w((lambda *s: torch.randn(*s, dtype=torch.bfloat16) * 3.2)(Hq * Dh, D) * ((Hq * Dh) ** -0.5)),
        }
    _A_in = _A_setup()

    def _B_setup():
        B, S, D, F = 8, 1, 512, 1792
        return {
            "x": _w((lambda *s: torch.randn(*s, dtype=torch.bfloat16) * 0.05)(B, S, D)),
            "w_gate": _w((lambda *s: torch.randn(*s, dtype=torch.bfloat16) * 0.4)(D, F) * (D ** -0.5)),
            "w_up": _w((lambda *s: torch.randn(*s, dtype=torch.bfloat16) * 0.4)(D, F) * (D ** -0.5)),
            "w_down": _w((lambda *s: torch.randn(*s, dtype=torch.bfloat16) * 0.4)(F, D) * (F ** -0.5)),
        }
    _B_in = _B_setup()

    return {
        "A_x": _A_in["x"],
        "A_k_cache": _A_in["k_cache"],
        "A_v_cache": _A_in["v_cache"],
        "A_wq": _A_in["wq"],
        "A_wo": _A_in["wo"],
        "B_x": _B_in["x"],
        "B_w_gate": _B_in["w_gate"],
        "B_w_up": _B_in["w_up"],
        "B_w_down": _B_in["w_down"],
    }

def reference(A_x, A_k_cache, A_v_cache, A_wq, A_wo, B_x, B_w_gate, B_w_up, B_w_down):
    def _A_ref(x, k_cache, v_cache, wq, wo):
        B, Sq, D = x.shape
        Hq, Dh = 32, 128
        Hkv, Sk = k_cache.shape[1], k_cache.shape[2]
        W = min(4096, max(256, Sk // 4))
        rep = Hq // Hkv
        q = (lambda x, *d: x.permute(*d))((x @ wq).reshape(B, Sq, Hq, Dh), 0, 2, 1, 3)
        k = (lambda x, n, axis: torch.repeat_interleave(x, n, dim=axis))(k_cache[:, :, Sk - W:, :], rep, axis=1)
        v = (lambda x, n, axis: torch.repeat_interleave(x, n, dim=axis))(v_cache[:, :, Sk - W:, :], rep, axis=1)
        scores = torch.einsum("bhqd,bhkd->bhqk", q, k) * (Dh ** -0.5)
        p = _softmax(scores, axis=-1)
        o = torch.einsum("bhqk,bhkd->bhqd", p, v)
        return (lambda x, *d: x.permute(*d))(o, 0, 2, 1, 3).reshape(B, Sq, Hq * Dh) @ wo

    def _B_ref(x, w_gate, w_up, w_down):
        g = torch.einsum("bsd,df->bsf", x, w_gate)
        u = torch.einsum("bsd,df->bsf", x, w_up)
        h = g * u
        return torch.einsum("bsf,fd->bsd", h, w_down)

    x = A_x
    xn = _layernorm(x, axis=-1)
    s = _A_ref(xn, A_k_cache, A_v_cache, A_wq, A_wo)
    g = _silu(_B_ref(xn, B_w_gate, B_w_up, B_w_down))
    return x + s * g
```

```cpp
#include <hip/hip_runtime.h>
#include <hip/hip_bf16.h>
#include <stdint.h>
#define EMU 0
typedef unsigned short u16;
__device__ __forceinline__ float bf2f(u16 v){ return __uint_as_float(((unsigned)v)<<16); }
__device__ __forceinline__ u16 f2bf(float f){ unsigned u=__float_as_uint(f); u += 0x7FFFu + ((u>>16)&1u); return (u16)(u>>16); }
__device__ __forceinline__ float rb(float f){
  return f;
}
constexpr int B=8, D=512, HQ=32, HKV=8, SK=4096, DH=128, W=1024, F=1792;
constexpr int WS_Q=0;
constexpr int WS_G=WS_Q+B*4096;
constexpr int WS_U=WS_G+B*F;
constexpr int WS_O=WS_U+B*F;

__device__ void ln_rows(const u16* x, float* xn  ){
  int tid=threadIdx.x; int wave=tid>>6, lane=tid&63;
  for(int r=wave;r<B;r+=blockDim.x/64){
    float v[8]; float s=0;
    for(int i=0;i<8;i++){ v[i]=bf2f(x[r*D+lane*8+i]); s+=v[i]; }
    for(int o=32;o;o>>=1) s+=__shfl_xor(s,o);
    float mean=s/D; float q=0;
    for(int i=0;i<8;i++){ float d=v[i]-mean; q+=d*d; }
    for(int o=32;o;o>>=1) q+=__shfl_xor(q,o);
    float var=q/D;
    float mb=rb(mean), vb=rb(var);
    float den=rb(sqrtf(rb(vb+1e-5f)));
    for(int i=0;i<8;i++) xn[r*D+lane*8+i]=rb(rb(v[i]-mb)/den);
  }
  __syncthreads();
}
__global__ void k1(const u16* x,const u16* wq,const u16* wg,const u16* wu,float* ws){
  __shared__ float xn[B*D];
  __shared__ float red[4][B][64];
  ln_rows(x,xn);
  int col0=blockIdx.x*64; const u16* w; int ld; float* out; int c;
  if(col0<4096){ w=wq; ld=4096; out=ws+WS_Q; c=col0; }
  else if(col0<4096+F){ w=wg; ld=F; out=ws+WS_G; c=col0-4096; }
  else { w=wu; ld=F; out=ws+WS_U; c=col0-4096-F; }
  int lane=threadIdx.x&63, kg=threadIdx.x>>6;
  float acc[B]; for(int b=0;b<B;b++) acc[b]=0;
  for(int k=kg*128;k<kg*128+128;k++){ float wv=bf2f(w[(size_t)k*ld+c+lane]); for(int b=0;b<B;b++) acc[b]+=xn[b*D+k]*wv; }
  for(int b=0;b<B;b++) red[kg][b][lane]=acc[b];
  __syncthreads();
  for(int i=threadIdx.x;i<B*64;i+=256){ int b=i>>6,l=i&63; float s=red[0][b][l]+red[1][b][l]+red[2][b][l]+red[3][b][l]; out[b*ld+c+l]=rb(s); }
}
__global__ void k2(const u16* kc,const u16* vc,float* ws){
  __shared__ float q[DH]; __shared__ float sc[W]; __shared__ float red[8]; __shared__ float oacc[2][DH];
  int b=blockIdx.x/HQ, h=blockIdx.x%HQ, kvh=h/4; int tid=threadIdx.x;
  if(tid<DH) q[tid]=ws[WS_Q+b*4096+h*DH+tid];
  __syncthreads();
  const u16* kb=kc+((size_t)(b*HKV+kvh)*SK+(SK-W))*DH; const u16* vb=vc+((size_t)(b*HKV+kvh)*SK+(SK-W))*DH;
  float mx=-INFINITY;
  for(int j=tid;j<W;j+=256){ float s=0; for(int d=0;d<DH;d++) s+=q[d]*bf2f(kb[(size_t)j*DH+d]); s=rb(rb(s)*0.08838834764831845f); sc[j]=s; mx=fmaxf(mx,s);}
  for(int o=32;o;o>>=1) mx=fmaxf(mx,__shfl_xor(mx,o));
  if((tid&63)==0) red[tid>>6]=mx; __syncthreads();
  mx=fmaxf(fmaxf(red[0],red[1]),fmaxf(red[2],red[3])); __syncthreads();
  float sum=0; for(int j=tid;j<W;j+=256){ float e=expf(sc[j]-mx); sc[j]=e; sum+=e; }
  for(int o=32;o;o>>=1) sum+=__shfl_xor(sum,o);
  if((tid&63)==0) red[tid>>6]=sum; __syncthreads();
  sum=red[0]+red[1]+red[2]+red[3];
  for(int j=tid;j<W;j+=256) sc[j]=rb(sc[j]/sum);
  __syncthreads();
  int d=tid&127, half=tid>>7; float a=0;
  for(int j=half*512;j<half*512+512;j++) a+=sc[j]*bf2f(vb[(size_t)j*DH+d]);
  oacc[half][d]=a; __syncthreads();
  if(tid<DH) ws[WS_O+b*4096+h*DH+tid]=rb(oacc[0][tid]+oacc[1][tid]);
}
__global__ void k3(const u16* x,const u16* wo,const u16* wd,const float* ws,u16* out){
  __shared__ float r1[4][64], r2[4][64];
  int b=blockIdx.x>>3, c0=(blockIdx.x&7)*64; int lane=threadIdx.x&63, kg=threadIdx.x>>6;
  float s=0; for(int k=kg*1024;k<kg*1024+1024;k++) s+=ws[WS_O+b*4096+k]*bf2f(wo[(size_t)k*D+c0+lane]);
  float d=0; for(int k=kg*448;k<kg*448+448;k++){ float h=rb(ws[WS_G+b*F+k]*ws[WS_U+b*F+k]); d+=h*bf2f(wd[(size_t)k*D+c0+lane]); }
  r1[kg][lane]=s; r2[kg][lane]=d; __syncthreads();
  if(threadIdx.x<64){ s=rb(r1[0][lane]+r1[1][lane]+r1[2][lane]+r1[3][lane]); d=rb(r2[0][lane]+r2[1][lane]+r2[2][lane]+r2[3][lane]);
    float g=rb(d/(1.f+expf(-d))); float o=bf2f(x[b*D+c0+lane])+rb(s*g); out[b*D+c0+lane]=f2bf(o); }
}
extern "C" void kernel_launch(void* const* d_in,const int* in_sizes,int n_in,void* d_out,int out_size,void* d_ws,size_t ws_size,hipStream_t stream){
  const u16* x=(const u16*)d_in[0]; const u16* kc=(const u16*)d_in[1]; const u16* vc=(const u16*)d_in[2];
  const u16* wq=(const u16*)d_in[3]; const u16* wo=(const u16*)d_in[4];
  const u16* wg=(const u16*)d_in[6]; const u16* wu=(const u16*)d_in[7]; const u16* wd=(const u16*)d_in[8];
  float* ws=(float*)d_ws;
  k1<<<120,256,0,stream>>>(x,wq,wg,wu,ws);
  k2<<<B*HQ,256,0,stream>>>(kc,vc,ws);
  k3<<<64,256,0,stream>>>(x,wo,wd,ws,(u16*)d_out);
}
```

```cpp
#include <hip/hip_runtime.h>
#include <stdint.h>

typedef unsigned short u16;
typedef unsigned v4u __attribute__((ext_vector_type(4)));
typedef unsigned v2u __attribute__((ext_vector_type(2)));
typedef float f4 __attribute__((ext_vector_type(4)));
typedef short s4v __attribute__((ext_vector_type(4)));
#define GAS __attribute__((address_space(1)))
#define LAS __attribute__((address_space(3)))

#define PACE 272
#define PACE2 320
#define KSTART 215
constexpr int PRE = 1;
constexpr int VDEPTH = 16;
constexpr int LAG = 5;
constexpr int LAGV = 2;
constexpr int GUAT = 4;
constexpr int SKEW = 11;
constexpr int QDELAY = 12, QSLEEP = 4, ODELAY = 4, SDELAY = 8, PSTAG = 4;

constexpr int DM = 512, SK = 4096, DH = 128, WIN = 1024, FF = 1792, NQ = 4096;
constexpr int NWG = 256, NTHR = 1024;
constexpr int WS_E = 0;
constexpr int WS_Q = 4096;
constexpr int WS_GU = WS_Q + 2 * 8 * 32768;
constexpr int WS_OP = WS_GU + 4 * 8 * 14336;
constexpr int WS_ST = WS_OP + 64 * 4 * 4 * 512;
constexpr int WS_SP = WS_ST + 64 * 4 * 4 * 16;
constexpr int WS_DP = WS_SP + 32 * 8 * 4096;
constexpr int WS_END = WS_DP + 32 * 8 * 4096;
constexpr int L_K = 0;
constexpr int L_V = 65536;
constexpr int L_W1 = 122880;
constexpr int L_WD = L_W1 + 8192;
constexpr int L_WO = L_W1 + 16384;
constexpr int XN_LD = 528;
constexpr int L_XN = L_V + 8192;
constexpr int QS_LD = 272;
constexpr int L_QS = 155648;
constexpr int L_RST = 156800;
constexpr int OS_LD = 272;
constexpr int L_OS = 157056;
constexpr int HS_LD = 144;
constexpr int L_HS = 159232;
constexpr int L_FLG = 160384;
constexpr int L_PF = 160512;
constexpr int LDS_BYTES = 162560;
static_assert(L_V + 7 * 8192 == L_W1, "V tile 7 = start of the phase-1 region");
enum { F_XN = 0, F_P1 = 1, F_Q = 2, F_H = 5, F_TAG = 8, F_WL = 11, F_D = 12, F_S = 13, F_G = 14,
       F_VT = 16  , F_VI = 20  , F_OB = 24  , F_WDL = 28   };
constexpr unsigned SPIN_CAP = 1u << 20;

struct Params { const u16 *x, *k, *v, *wq, *wo, *wg, *wu, *wd; u16* out; unsigned char* ws; };

__device__ __forceinline__ unsigned pk2(float lo, float hi) {
  typedef __bf16 bf2 __attribute__((ext_vector_type(2)));
  bf2 r; r[0] = (__bf16)lo; r[1] = (__bf16)hi; return __builtin_bit_cast(unsigned, r);
}
__device__ __forceinline__ float lo2f(unsigned v) { return __uint_as_float(v << 16); }
__device__ __forceinline__ float hi2f(unsigned v) { return __uint_as_float(v & 0xffff0000u); }
__device__ __forceinline__ float rb(float f) { return (float)(__bf16)f; }
__device__ __forceinline__ f4 mfma(v4u a, v4u b, f4 c) {
  return __builtin_amdgcn_mfma_f32_16x16x32_bf16(__builtin_bit_cast(__attribute__((ext_vector_type(8))) __bf16, a),
                                                 __builtin_bit_cast(__attribute__((ext_vector_type(8))) __bf16, b), c, 0, 0, 0);
}
__device__ __forceinline__ v2u trd(LAS unsigned char* p) {
  return __builtin_bit_cast(v2u, __builtin_amdgcn_ds_read_tr16_b64_v4i16((LAS s4v*)p));
}
__device__ __forceinline__ void dma16(const void* g, LAS unsigned char* l) {
  __builtin_amdgcn_global_load_lds((const GAS void*)g, (LAS void*)l, 16, 0, 2);
}
__device__ __forceinline__ void lds_arrive(LAS unsigned* c, int lane) {
  asm volatile("" ::: "memory");
  if (lane == 0) __hip_atomic_fetch_add(c, 1u, __ATOMIC_RELAXED, __HIP_MEMORY_SCOPE_WORKGROUP);
  asm volatile("" ::: "memory");
}
__device__ __forceinline__ void lds_wait(LAS unsigned* c, unsigned target) {
  while (__hip_atomic_load(c, __ATOMIC_RELAXED, __HIP_MEMORY_SCOPE_WORKGROUP) < target) __builtin_amdgcn_s_sleep(1);
  asm volatile("" ::: "memory");
}
__device__ __forceinline__ void lds_wait_fast(LAS unsigned* c, unsigned target) {
  while (__hip_atomic_load(c, __ATOMIC_RELAXED, __HIP_MEMORY_SCOPE_WORKGROUP) < target) {}
  asm volatile("" ::: "memory");
}
__device__ __forceinline__ void lds_set_asm(unsigned addr, unsigned val) {
  asm volatile("ds_write_b32 %0, %1" ::"v"(addr), "v"(val) : "memory");
}
__device__ __forceinline__ void lds_wait_asm(unsigned addr, unsigned target) {
  for (;;) {
    unsigned v;
    asm volatile("ds_read_b32 %0, %1\n\ts_waitcnt lgkmcnt(0)" : "=v"(v) : "v"(addr) : "memory");
    if (__builtin_amdgcn_readfirstlane(v) >= target) break;
    __builtin_amdgcn_s_sleep(1);
  }
}
__device__ __forceinline__ unsigned cvtpk(float lo, float hi) { unsigned r = pk2(lo, hi); asm volatile("" : "+v"(r)); return r; }
__device__ __forceinline__ float max3(float a, float b, float c) { return fmaxf(fmaxf(a, b), c); }
#define PIN(x) asm volatile("" : "+v"(x))
__device__ __forceinline__ unsigned now32() { unsigned long long t; asm volatile("s_memrealtime %0\n\ts_waitcnt lgkmcnt(0)" : "=s"(t)::"memory"); return (unsigned)t; }
template <int CTRL> __device__ __forceinline__ float dpp(float x) {
  return __builtin_bit_cast(float, __builtin_amdgcn_mov_dpp(__builtin_bit_cast(int, x), CTRL, 0xf, 0xf, true));
}
template <int M> __device__ __forceinline__ void xpair(float v, float& a, float& b) {
  if constexpr (M == 1) { a = v; b = dpp<0xB1>(v); }
  else if constexpr (M == 2) { a = v; b = dpp<0x4E>(v); }
  else if constexpr (M == 4) { a = v; b = dpp<0x141>(v); }
  else if constexpr (M == 8) { a = v; b = dpp<0x128>(v); }
  else if constexpr (M == 16) { auto r = __builtin_amdgcn_permlane16_swap(__float_as_uint(v), __float_as_uint(v), false, false); a = __uint_as_float(r[0]); b = __uint_as_float(r[1]); }
  else { auto r = __builtin_amdgcn_permlane32_swap(__float_as_uint(v), __float_as_uint(v), false, false); a = __uint_as_float(r[0]); b = __uint_as_float(r[1]); }
}
template <int M> __device__ __forceinline__ float xsumT(float v) { float a, b; xpair<M>(v, a, b); return a + b; }
template <int M> __device__ __forceinline__ float xmaxT(float v) { float a, b; xpair<M>(v, a, b); return fmaxf(a, b); }
#define xsum(v, m) xsumT<m>(v)
#define xmax(v, m) xmaxT<m>(v)
__device__ __forceinline__ float wsum(float s) { s = xsum(s, 1); s = xsum(s, 2); s = xsum(s, 4); s = xsum(s, 8); s = xsum(s, 16); s = xsum(s, 32); return s; }
#define SC1 16
#define LD_SC1(off) __builtin_amdgcn_raw_buffer_load_b128(rs, (off), 0, SC1)
#define ST_SC1(val, off) __builtin_amdgcn_raw_buffer_store_b128((val), rs, (off), 0, SC1)
__device__ __forceinline__ v4u gran(float a, float b, unsigned tag) { v4u r = {__float_as_uint(a), tag, __float_as_uint(b), tag}; return r; }
#define TAGBAD(u) (((u)[1] ^ tag) | ((u)[3] ^ tag))
#define F0(u) __uint_as_float((u)[0])
#define F2(u) __uint_as_float((u)[2])
template <int N, int SLEEP>
__device__ __forceinline__ void gather(v4u (&a)[N], __amdgpu_buffer_rsrc_t rs, int off, int stride, unsigned tag) {
#pragma unroll
  for (int i = 0; i < N; ++i) a[i] = LD_SC1(off + i * stride);
  _Pragma("nounroll") for (unsigned spin = 0; spin < SPIN_CAP; ++spin) {
    unsigned bad = 0u;
#pragma unroll
    for (int i = 0; i < N; ++i) bad |= TAGBAD(a[i]);
    if (__all(bad == 0u)) break;
    if (SLEEP) __builtin_amdgcn_s_sleep(SLEEP);
    asm volatile("" ::: "memory");
#pragma unroll
    for (int i = 0; i < N; ++i) if (TAGBAD(a[i]) != 0u) a[i] = LD_SC1(off + i * stride);
  }
}

template <int N, int SLEEP>
__device__ __forceinline__ void gather_p(unsigned (&f0)[N], unsigned (&f2)[N], __amdgpu_buffer_rsrc_t rs, const int (&off_in)[N], unsigned tag) {
  int off[N];
  v4u a[N], b[N];
#pragma unroll
  for (int i = 0; i < N; ++i) { off[i] = off_in[i]; f0[i] = 0u; f2[i] = 0u; a[i] = LD_SC1(off[i]); }
#define GP_MERGE(x) do { int all = 0x7fffffff; \
    _Pragma("unroll") for (int i = 0; i < N; ++i) { const bool ok = TAGBAD(x[i]) == 0u; f0[i] = ok ? x[i][0] : f0[i]; f2[i] = ok ? x[i][2] : f2[i]; \
      off[i] = ok ? 0x7fffffff : off[i]; all &= off[i]; } \
    if (__all(all == 0x7fffffff)) return; } while (0)
  _Pragma("nounroll") for (unsigned spin = 0; spin < SPIN_CAP; ++spin) {
    __builtin_amdgcn_s_sleep(SLEEP);
    asm volatile("" ::: "memory");
#pragma unroll
    for (int i = 0; i < N; ++i) b[i] = LD_SC1(off[i]);
    asm volatile("" ::: "memory");
    GP_MERGE(a);
    __builtin_amdgcn_s_sleep(SLEEP);
    asm volatile("" ::: "memory");
#pragma unroll
    for (int i = 0; i < N; ++i) a[i] = LD_SC1(off[i]);
    asm volatile("" ::: "memory");
    GP_MERGE(b);
  }
#undef GP_MERGE
}

#define THRN(n) asm volatile("s_waitcnt vmcnt(%0)" ::"i"(n) : "memory")

__device__ __forceinline__ v4u pair_unit(f4 v, bool h, bool par, unsigned tag) {
  const float lo = h ? v[2] : v[0], hi = h ? v[3] : v[1];
  const float mine = par ? hi : lo, send = par ? lo : hi;
  const float recv = dpp<0xB1>(send);
  return gran(par ? recv : mine, par ? mine : recv, tag);
}

__global__ void __launch_bounds__(NTHR, 4) fused_decode(Params p) {
  extern __shared__ __attribute__((aligned(16))) unsigned char smem_g[];
  LAS unsigned char* sm = (LAS unsigned char*)smem_g;
  LAS unsigned* flg = (LAS unsigned*)(sm + L_FLG);
  const int tid = threadIdx.x, lane = tid & 63;
  const int pw = __builtin_amdgcn_readfirstlane(tid >> 6);
  const int wv = pw < 8 ? 12 - (pw & 4) + (pw & 3) : pw - 8;
  const int bid = blockIdx.x, xcd = bid & 7, idx = bid >> 3, w = xcd * 32 + idx;
  const int l15 = lane & 15, g = lane >> 4;
  const int qq = l15 >> 2, pp = lane & 3;
  const int r0 = 4 * g + qq;
  const __amdgpu_buffer_rsrc_t rs = __builtin_amdgcn_make_buffer_rsrc(p.ws, 0, WS_END, 0x00020000);
  constexpr float LOG2E = 1.4426950408889634f;
  const unsigned t_start = (unsigned)__builtin_amdgcn_s_memrealtime() - (unsigned)((idx * SKEW) >> 4);
  if (wv == 0 && lane < 32) flg[lane] = 0u;
  if (wv == 1) ((LAS unsigned*)(sm + L_RST))[lane] = 0xFFFFFFFFu;
#define START_BARRIER() do { asm volatile("s_waitcnt lgkmcnt(0)" ::: "memory"); __builtin_amdgcn_s_barrier(); asm volatile("" ::: "memory"); } while (0)
  v4u xr = {0u, 0u, 0u, 0u};
  unsigned ep = 0;
  if (wv < 8) {
    xr = *(const v4u*)(p.x + wv * DM + lane * 8);
    ep = *(const unsigned*)(p.ws + WS_E);
    asm volatile("" ::: "memory");
  }
  if (wv < 12) START_BARRIER();
  const int pair = w >> 2, split = w & 3;
  const size_t kvbase = ((size_t)pair * SK + (SK - WIN) + split * 256) * DH;
  const int H = w >> 3, cb8 = w & 7;
  const bool is_q = idx < 16, is_p1 = idx < 30;
  const int blk = is_q ? xcd * 16 + idx : xcd * 14 + (idx - 16);
  const int rh = blk & 1;

  if (wv < 8) {
    {
      float v[8];
#pragma unroll
      for (int i = 0; i < 4; ++i) { v[2 * i] = lo2f(xr[i]); v[2 * i + 1] = hi2f(xr[i]); }
      float s = ((v[0] + v[1]) + (v[2] + v[3])) + ((v[4] + v[5]) + (v[6] + v[7]));
      s = wsum(s);
      const float mean = s * (1.f / DM);
      float q = 0.f;
#pragma unroll
      for (int i = 0; i < 8; ++i) { const float d = v[i] - mean; q = fmaf(d, d, q); }
      q = wsum(q);
      const float mb = rb(mean), vb = rb(q * (1.f / DM));
      const float den = rb(sqrtf(rb(vb + 1e-5f)));
      const float rc = __builtin_amdgcn_rcpf(den);
      unsigned y[4];
#pragma unroll
      for (int i = 0; i < 4; ++i) {
        const unsigned c = pk2(v[2 * i] - mb, v[2 * i + 1] - mb);
        const float a0 = lo2f(c), a1 = hi2f(c);
        float q0 = a0 * rc, q1 = a1 * rc;
        q0 = fmaf(fmaf(-q0, den, a0), rc, q0); q1 = fmaf(fmaf(-q1, den, a1), rc, q1);
        y[i] = pk2(q0, q1);
      }
      if ((lane >> 5) == rh) {
        const int k0 = (lane & 31) * 8, bk = k0 >> 5, hi = (k0 >> 4) & 1, gq = (k0 >> 2) & 3;
        LAS unsigned char* dst = sm + L_XN + wv * XN_LD + bk * 64 + hi * 8;
        *(LAS v2u*)(dst + gq * 16) = v2u{y[0], y[1]};
        *(LAS v2u*)(dst + (gq + 1) * 16) = v2u{y[2], y[3]};
      }
    }
    if (wv == 0 && lane == 0) flg[F_TAG] = (ep + 0x9E3779BAu) | 1u;
    lds_arrive(flg + F_XN, lane);
    lds_wait(flg + F_VT + (wv >> 1), 1);
    v4u kf[2][4];
#pragma unroll
    for (int t = 0; t < 2; ++t)
#pragma unroll
      for (int ks = 0; ks < 4; ++ks)
        kf[t][ks] = *(LAS v4u*)(sm + L_K + (wv * 32 + t * 16 + l15) * 256 + (((ks * 4 + g) ^ ((l15 & 7) << 1)) * 16));
    unsigned qaddr = (unsigned)(size_t)(sm + L_QS + (l15 & 3) * QS_LD + g * 16);
    unsigned radd = (unsigned)(size_t)(sm + L_RST + (l15 & 3) * 32);
    unsigned gadd = (unsigned)(size_t)(sm + L_RST + 128 + g * 32);
    unsigned pwadd = (unsigned)(size_t)(sm + L_PF + wv * 256 + (g * 4 + (l15 & 3)) * 16);
    unsigned pradd = (unsigned)(size_t)(sm + L_PF + (g * 4 + qq) * 16);
    unsigned vadd = (unsigned)(size_t)(sm + L_V + r0 * 256 + ((wv ^ (r0 & 7)) * 32) + pp * 8);
    int ooff = WS_OP + ((pair * 4 + split) * 4 + g) * 512 + (wv * 4 + qq) * 16;
    PIN(qaddr); PIN(radd); PIN(gadd); PIN(pwadd); PIN(pradd); PIN(vadd); PIN(ooff);
    lds_wait(flg + F_XN, 12);
    unsigned tag = flg[F_TAG];
    PIN(tag);
    lds_wait(flg + F_Q, 4);
    v4u qf[4];
#pragma unroll
    for (int ks = 0; ks < 4; ++ks) qf[ks] = *(LAS v4u*)(qaddr + ks * 64);
    f4 sc[2];
#pragma unroll
    for (int t = 0; t < 2; ++t) {
      f4 a = {0.f, 0.f, 0.f, 0.f};
#pragma unroll
      for (int ks = 0; ks < 4; ++ks) a = mfma(kf[t][ks], qf[ks], a);
      sc[t] = a;
    }
    float m = -INFINITY;
#pragma unroll
    for (int t = 0; t < 2; ++t)
#pragma unroll
      for (int r = 0; r < 4; r += 2) {
        const unsigned a = cvtpk(sc[t][r], sc[t][r + 1]);
        const unsigned b = cvtpk(lo2f(a) * 0.08838834764831845f, hi2f(a) * 0.08838834764831845f);
        sc[t][r] = lo2f(b); sc[t][r + 1] = hi2f(b);
        m = max3(m, sc[t][r], sc[t][r + 1]);
      }
    { float u, v; xpair<16>(m, u, v); m = max3(u, v, v); xpair<32>(m, u, v); m = max3(u, v, v); }
    *(LAS float*)(radd + wv * 4) = m;
    float M = 0.f;
    for (unsigned spin = 0; spin < SPIN_CAP; ++spin) {
      const v4u x = *(volatile LAS v4u*)radd, y = *(volatile LAS v4u*)(radd + 16);
      const unsigned mx = max(max(max(x[0], x[1]), max(x[2], x[3])), max(max(y[0], y[1]), max(y[2], y[3])));
      if (__all(mx != 0xFFFFFFFFu)) {
        M = max3(max3(__uint_as_float(x[0]), __uint_as_float(x[1]), __uint_as_float(x[2])), max3(__uint_as_float(x[3]), __uint_as_float(y[0]), __uint_as_float(y[1])),
                 max3(__uint_as_float(y[2]), __uint_as_float(y[3]), __uint_as_float(y[3])));
        break;
      }
    }
    const float Ml = M * LOG2E;
#pragma unroll
    for (int t = 0; t < 2; ++t)
#pragma unroll
      for (int r = 0; r < 4; ++r) sc[t][r] = __builtin_amdgcn_exp2f(fmaf(sc[t][r], LOG2E, -Ml));
    *(LAS v4u*)pwadd = v4u{cvtpk(sc[0][0], sc[0][1]), cvtpk(sc[0][2], sc[0][3]), cvtpk(sc[1][0], sc[1][1]), cvtpk(sc[1][2], sc[1][3])};
    float l = ((sc[0][0] + sc[0][1]) + (sc[0][2] + sc[0][3])) + ((sc[1][0] + sc[1][1]) + (sc[1][2] + sc[1][3]));
    l = xsum(l, 16); l = xsum(l, 32);
    asm volatile("" ::: "memory");
    *(LAS float*)(radd + 128 + wv * 4) = l;
    float L = 1.f;
    for (unsigned spin = 0; spin < SPIN_CAP; ++spin) {
      const v4u x = *(volatile LAS v4u*)gadd, y = *(volatile LAS v4u*)(gadd + 16);
      const unsigned mx = max(max(max(x[0], x[1]), max(x[2], x[3])), max(max(y[0], y[1]), max(y[2], y[3])));
      if (__all(mx != 0xFFFFFFFFu)) {
        L = ((__uint_as_float(x[0]) + __uint_as_float(x[1])) + (__uint_as_float(x[2]) + __uint_as_float(x[3]))) +
            ((__uint_as_float(y[0]) + __uint_as_float(y[1])) + (__uint_as_float(y[2]) + __uint_as_float(y[3])));
        break;
      }
    }
    asm volatile("" ::: "memory");
    v4u pf[8];
#pragma unroll
    for (int T = 0; T < 8; ++T) pf[T] = *(LAS v4u*)(pradd + T * 256);
    if (wv == 0 && l15 == g) ST_SC1(gran(M, L, tag), WS_ST + ((pair * 4 + split) * 4 + g) * 16);
    float invl = __builtin_amdgcn_rcpf(L);
    invl = fmaf(fmaf(-L, invl, 1.f), invl, invl);
    PIN(invl);
    f4 acc0 = {0.f, 0.f, 0.f, 0.f}, acc1 = {0.f, 0.f, 0.f, 0.f};
    const int slot0 = ((wv * 8 + (H >> 2)) * 4) * 4 + (H & 3);
    const int soff = WS_ST + (slot0 + (lane & 3) * 4) * 16;
    v4u st0 = {0u, 0u, 0u, 0u};
#pragma unroll
    for (int hh = 0; hh < 2; ++hh) {
      for (;;) {
        const v4u pr = *(volatile LAS v4u*)(flg + F_VT);
        if (__builtin_amdgcn_readfirstlane(min(min(pr[0], pr[1]), min(pr[2], pr[3]))) >= 2u + hh) break;
        __builtin_amdgcn_s_sleep(1);
      }
      asm volatile("" ::: "memory");
      if (hh == 1) st0 = LD_SC1(soff);
      v2u lo[4], hi[4];
#pragma unroll
      for (int j = 0; j < 4; ++j) { lo[j] = trd((LAS unsigned char*)(vadd + (2 * j + hh) * 8192)); hi[j] = trd((LAS unsigned char*)(vadd + (2 * j + hh) * 8192 + 4096)); }
#pragma unroll
      for (int j = 0; j < 4; ++j) {
        if (j & 1) acc1 = mfma(pf[2 * j + hh], v4u{lo[j][0], lo[j][1], hi[j][0], hi[j][1]}, acc1);
        else acc0 = mfma(pf[2 * j + hh], v4u{lo[j][0], lo[j][1], hi[j][0], hi[j][1]}, acc0);
      }
    }
    {
      const float x = (acc0[0] + acc1[0]) * invl;
      const float y = dpp<0xB1>(x);
      const unsigned d = cvtpk(x, y);
      const unsigned e = __builtin_amdgcn_mov_dpp(d, 0x4E, 0xf, 0xf, true);
      if (pp == 0) ST_SC1((v4u{d, tag, e, tag}), ooff);
    }
    {
      v4u st[1] = {st0};
      if (!__all(TAGBAD(st0) == 0u)) gather<1, 2>(st, rs, soff, 0, tag);
      float Mx = F0(st[0]);
      Mx = xmax(Mx, 1); Mx = xmax(Mx, 2);
      float e = __builtin_amdgcn_exp2f((F0(st[0]) - Mx) * LOG2E) * F2(st[0]);
      float Ls = xsum(e, 1); Ls = xsum(Ls, 2);
      e *= __builtin_amdgcn_rcpf(Ls);
      const bool up = lane >= 32;
      float c0 = up ? dpp<0x55>(e) : dpp<0x00>(e), c1 = up ? dpp<0xFF>(e) : dpp<0xAA>(e);
      const int k = (lane & 31) * 4;
      unsigned osadd = (unsigned)(size_t)(sm + L_OS + wv * OS_LD + ((k & ~31) + 8 * ((k >> 2) & 3) + 4 * ((k >> 4) & 1)) * 2);
      PIN(c0); PIN(c1); PIN(osadd);
      unsigned a0[2], a2[2];
      const int off = WS_OP + (slot0 + (lane >> 5) * 4) * 512 + (lane & 31) * 16;
      if (ODELAY) __builtin_amdgcn_s_sleep(ODELAY);
      const int offs[2] = {off, off + 4096};
      gather_p<2, PSTAG>(a0, a2, rs, offs, tag);
      float o0 = fmaf(c1, lo2f(a0[1]), c0 * lo2f(a0[0])), o1 = fmaf(c1, hi2f(a0[1]), c0 * hi2f(a0[0]));
      float o2 = fmaf(c1, lo2f(a2[1]), c0 * lo2f(a2[0])), o3 = fmaf(c1, hi2f(a2[1]), c0 * hi2f(a2[0]));
      o0 = xsum(o0, 32); o1 = xsum(o1, 32); o2 = xsum(o2, 32); o3 = xsum(o3, 32);
      *(LAS v2u*)osadd = v2u{pk2(o0, o1), pk2(o2, o3)};
    }
    asm volatile("" ::: "memory");
    *(volatile LAS unsigned char*)((LAS unsigned char*)(flg + F_OB) + wv) = 1;
    return;
  }

  const int ww = wv & 3, wt = ww * 64 + lane;
  const int chunk8 = (lane & 7) ^ (((lane >> 4) & 3) << 1);
  if (wv < 12) {
    __builtin_amdgcn_s_setprio(2);
    const u16* kb = p.k + kvbase + (size_t)(ww * 64 + g) * DH;
    const u16* vb = p.v + kvbase + (size_t)(ww * 64 + g) * DH;
    const unsigned fbase = (unsigned)(size_t)flg;
    const unsigned fprog = fbase + (F_VT + ww) * 4;
#pragma unroll
    for (int i = 0; i < 38; ++i) {
      const unsigned tgt = t_start + KSTART + (unsigned)((i > PRE ? i - PRE : 0) * PACE + (i > 31 ? (i - 31) * (PACE2 - PACE) : 0)) / 16u;
      if (i == 0 || i > PRE) while ((int)(now32() - tgt) < 0) __builtin_amdgcn_s_sleep(1);
      if (i == 24) lds_wait_asm(fbase + F_P1 * 4, 4);
      if (i < 32) {
        const int chunk = l15 ^ ((((i & 1) * 4 + g) & 7) << 1);
        dma16((i < 16 ? kb : vb) + (i & 15) * 4 * DH + chunk * 8, sm + (i < 16 ? L_K : L_V) + (ww * 16 + (i & 15)) * 1024);
      } else if (i < 34) {
        const int ins = (i - 32) * 4 + ww, row = ins * 8 + (lane >> 3);
        dma16(p.wd + (size_t)(H * 56 + (row < 56 ? row : 55)) * DM + cb8 * 64 + chunk8 * 8, sm + L_WD + ins * 1024);
      } else {
        const int ins = (i - 34) * 4 + ww;
        dma16(p.wo + (size_t)(H * 128 + ins * 8 + (lane >> 3)) * DM + cb8 * 64 + chunk8 * 8, sm + L_WO + ins * 1024);
      }
      if (i == 15 + LAG) { THRN(LAG); lds_set_asm(fprog, 1u); }
      else if (i == 23 + LAG) { THRN(LAG); lds_set_asm(fprog, 2u); }
      else if (i == 31 + LAGV) { THRN(LAGV); lds_set_asm(fprog, 3u); }
      else THRN(VDEPTH);
      if (i == 16 + GUAT) lds_set_asm(fbase + (F_VI + ww) * 4, 1u);
    }
    THRN(4);
    lds_set_asm(fbase + (F_WDL + ww) * 4, 1u);
    THRN(0);
    lds_arrive(flg + F_WL, lane);
    if (ww >= 2) return;
    {
      __builtin_amdgcn_s_setprio(3);
      const int fb = w >> 5, ct = w & 31;
      const int foff = (ww ? WS_DP : WS_SP) + ((lane >> 3) * 8 + fb) * 4096 + (ct * 8 + (lane & 7)) * 16;
      const unsigned xv = *(const unsigned*)(p.x + fb * DM + ct * 16 + (lane & 7) * 2);
      LAS float* gl = (LAS float*)(sm + L_HS) + (lane & 7) * 2;
      lds_wait(flg + (ww ? F_D : F_S), 4);
      const unsigned tag = flg[F_TAG];
      if (SDELAY) __builtin_amdgcn_s_sleep(SDELAY);
      unsigned e0[4], e2[4];
      const int offs[4] = {foff, foff + 8 * 32768, foff + 16 * 32768, foff + 24 * 32768};
      gather_p<4, PSTAG>(e0, e2, rs, offs, tag);
      float s0 = (__uint_as_float(e0[0]) + __uint_as_float(e0[1])) + (__uint_as_float(e0[2]) + __uint_as_float(e0[3]));
      float s1 = (__uint_as_float(e2[0]) + __uint_as_float(e2[1])) + (__uint_as_float(e2[2]) + __uint_as_float(e2[3]));
      s0 = xsum(s0, 8); s0 = xsum(s0, 16); s0 = xsum(s0, 32);
      s1 = xsum(s1, 8); s1 = xsum(s1, 16); s1 = xsum(s1, 32);
      const unsigned sb = pk2(s0, s1);
      if (ww) {
        const float d0 = lo2f(sb), d1 = hi2f(sb);
        gl[0] = rb(d0 / (1.f + __builtin_amdgcn_exp2f(-d0 * LOG2E))); gl[1] = rb(d1 / (1.f + __builtin_amdgcn_exp2f(-d1 * LOG2E)));
        asm volatile("" ::: "memory");
        *(volatile LAS unsigned*)(flg + F_G) = 1u;
        return;
      }
      lds_wait_fast(flg + F_G, 1);
      const unsigned sg = pk2(lo2f(sb) * gl[0], hi2f(sb) * gl[1]);
      if (lane < 8) *(unsigned*)(p.out + fb * DM + ct * 16 + lane * 2) = pk2(lo2f(xv) + lo2f(sg), hi2f(xv) + hi2f(sg));
      if (bid == 0 && lane == 0) __hip_atomic_store((GAS unsigned*)(p.ws + WS_E), tag, __ATOMIC_RELAXED, __HIP_MEMORY_SCOPE_AGENT);
    }
    return;
  }

  __builtin_amdgcn_s_setprio(3);
  const int cbk0 = blk >> 1, mat = (!is_q && cbk0 >= 28) ? 1 : 0, cblk = cbk0 - 28 * mat;
  LAS unsigned char* wsrc = sm + r0 * 128 + ((ww ^ ((r0 >> 1) & 3)) * 32) + pp * 8;
  const int bb = 4 * (g & 1) + 2 * (g >> 1) + (lane & 1);
  if (is_p1) {
    if (!is_q) __builtin_amdgcn_s_sleep(16);
    const int pitch = is_q ? NQ : FF;
    const u16* b0 = (is_q ? p.wq : (mat ? p.wu : p.wg)) + cblk * 64;
    const unsigned voff = (unsigned)((rh * 256 + ww * 8 + (lane >> 3)) * pitch + chunk8 * 8);
#pragma unroll
    for (int i = 0; i < 8; ++i) dma16(b0 + (size_t)(i * 32) * pitch + voff, sm + L_W1 + (i * 4 + ww) * 1024);
  }
  START_BARRIER();
  THRN(0);
  lds_arrive(flg + F_XN, lane);
  lds_wait(flg + F_XN, 12);
  const unsigned tag = flg[F_TAG];
  if (is_p1) {
    f4 a0 = {0.f, 0.f, 0.f, 0.f}, a1 = {0.f, 0.f, 0.f, 0.f};
    LAS unsigned char* xsrc = sm + L_XN + (l15 & 7) * XN_LD + g * 16;
#pragma unroll
    for (int ks = 0; ks < 8; ++ks) {
      const v2u lo = trd(wsrc + L_W1 + ks * 4096), hi = trd(wsrc + L_W1 + ks * 4096 + 2048);
      const v4u xb = *(LAS v4u*)(xsrc + ks * 64);
      if (ks & 1) a1 = mfma(xb, v4u{lo[0], lo[1], hi[0], hi[1]}, a1); else a0 = mfma(xb, v4u{lo[0], lo[1], hi[0], hi[1]}, a0);
    }
    const int off = (is_q ? WS_Q + (rh * 8 + bb) * 32768 : WS_GU + ((mat * 2 + rh) * 8 + bb) * 14336) + (cblk * 32 + ww * 8 + (l15 >> 1)) * 16;
    ST_SC1(pair_unit(a0 + a1, g >> 1, lane & 1, tag), off);
  }
  lds_arrive(flg + F_P1, lane);
  __builtin_amdgcn_s_sleep(QDELAY);
  {
    v4u a[2];
    gather<2, QSLEEP>(a, rs, WS_Q + (pair >> 3) * 32768 + ((pair & 7) * 256 + wt) * 16, 8 * 32768, tag);
    *(LAS unsigned*)(sm + L_QS + ww * QS_LD + lane * 4) = pk2(F0(a[0]) + F0(a[1]), F2(a[0]) + F2(a[1]));
    lds_arrive(flg + F_Q, lane);
  }
  {
    lds_wait(flg + F_VI + ww, 1);
    {
      const int b = wt >> 5, u5 = wt & 31, uu = u5 < 28 ? u5 : 27;
      v4u a[4];
      gather<4, QSLEEP>(a, rs, WS_GU + b * 14336 + (H * 28 + uu) * 16, 8 * 14336, tag);
      const unsigned gg = pk2(F0(a[0]) + F0(a[1]), F2(a[0]) + F2(a[1])), u2 = pk2(F0(a[2]) + F0(a[3]), F2(a[2]) + F2(a[3]));
      const int k = u5 * 2;
      const int pos = (k & ~31) + 8 * ((k >> 2) & 3) + 4 * ((k >> 4) & 1) + (k & 3);
      *(LAS unsigned*)(sm + L_HS + b * HS_LD + pos * 2) = u5 < 28 ? pk2(lo2f(gg) * lo2f(u2), hi2f(gg) * hi2f(u2)) : 0u;
    }
    lds_arrive(flg + F_H, lane);
    lds_wait(flg + F_H, 4);
#pragma unroll
    for (int j = 0; j < 4; ++j) lds_wait(flg + F_WDL + j, 1);
    f4 a = {0.f, 0.f, 0.f, 0.f};
#pragma unroll
    for (int ks = 0; ks < 2; ++ks) {
      const v2u lo = trd(wsrc + L_WD + ks * 4096), hi = trd(wsrc + L_WD + ks * 4096 + 2048);
      const v4u hb = *(LAS v4u*)(sm + L_HS + (l15 & 7) * HS_LD + ks * 64 + g * 16);
      a = mfma(hb, v4u{lo[0], lo[1], hi[0], hi[1]}, a);
    }
    ST_SC1(pair_unit(a, g >> 1, lane & 1, tag), WS_DP + (H * 8 + bb) * 4096 + (cb8 * 32 + ww * 8 + (l15 >> 1)) * 16);
    lds_arrive(flg + F_D, lane);
  }
  lds_wait(flg + F_WL, 4);
  v4u wb[4];
#pragma unroll
  for (int ks = 0; ks < 4; ++ks) {
    const v2u lo = trd(wsrc + L_WO + ks * 4096), hi = trd(wsrc + L_WO + ks * 4096 + 2048);
    wb[ks] = v4u{lo[0], lo[1], hi[0], hi[1]};
  }
  while (__builtin_amdgcn_readfirstlane((*(volatile LAS v2u*)(flg + F_OB))[0] & (*(volatile LAS v2u*)(flg + F_OB))[1]) != 0x01010101u) {}
  asm volatile("" ::: "memory");
  {
    f4 a0 = {0.f, 0.f, 0.f, 0.f}, a1 = {0.f, 0.f, 0.f, 0.f};
#pragma unroll
    for (int ks = 0; ks < 4; ++ks) {
      const v4u oa = *(LAS v4u*)(sm + L_OS + (l15 & 7) * OS_LD + ks * 64 + g * 16);
      if (ks & 1) a1 = mfma(oa, wb[ks], a1); else a0 = mfma(oa, wb[ks], a0);
    }
    ST_SC1(pair_unit(a0 + a1, g >> 1, lane & 1, tag), WS_SP + (H * 8 + bb) * 4096 + (cb8 * 32 + ww * 8 + (l15 >> 1)) * 16);
  }
  lds_arrive(flg + F_S, lane);
}

extern "C" void kernel_launch(void* const* d_in, const int* in_sizes, int n_in, void* d_out, int out_size, void* d_ws, size_t ws_size,
                              hipStream_t stream) {
  Params p;
  p.x = (const u16*)d_in[0]; p.k = (const u16*)d_in[1]; p.v = (const u16*)d_in[2]; p.wq = (const u16*)d_in[3]; p.wo = (const u16*)d_in[4];
  p.wg = (const u16*)d_in[6]; p.wu = (const u16*)d_in[7]; p.wd = (const u16*)d_in[8];
  p.out = (u16*)d_out; p.ws = (unsigned char*)d_ws;
  (void)hipFuncSetAttribute((const void*)fused_decode, hipFuncAttributeMaxDynamicSharedMemorySize, LDS_BYTES);
  fused_decode<<<NWG, NTHR, LDS_BYTES, stream>>>(p);
}
```
